```python
import torch

def _softmax(x, axis=-1):
    return torch.softmax(x, dim=axis)
def _gelu(x):
    return torch.nn.functional.gelu(x, approximate="tanh")
def _silu(x):
    return torch.nn.functional.silu(x)
def _sigmoid(x):
    return torch.sigmoid(x)
def _layernorm(x, axis=-1, eps=1e-5):
    m = x.mean(dim=axis, keepdim=True)
    v = x.var(dim=axis, keepdim=True, unbiased=False)
    return (x - m) / torch.sqrt(v + eps)
def _scatter_add(tgt, idx, src, axis=0):
    return tgt.clone().index_add_(axis, idx, src)
def _segment_sum(src, seg, n):
    out = torch.zeros((n,) + src.shape[1:], dtype=src.dtype)
    return out.index_add_(0, seg, src)
def _pad(x, pw):
    flat = tuple(v for pair in reversed(pw) for v in pair)
    return torch.nn.functional.pad(x, flat)

kernel_name = 'sequential_preLN__ssm_conv_s4d__mixer_token_mlp__prompt'

def setup_inputs():

    _w = (lambda a: (lambda t: t.to(torch.bfloat16) if t.is_floating_point() else t)(torch.as_tensor(a)))
    def _A_setup():
        Bsz, L, D, N = 1, 2048, 640, 192
        return {
            "u": _w((lambda *s: torch.randn(*s, dtype=torch.bfloat16) * 0.05)(Bsz, L, D)),
            "log_A_real": _w((lambda *s: torch.randn(*s, dtype=torch.bfloat16) * 6.4)(D, N) * 0.5),
            "A_imag": _w((lambda *s: torch.randn(*s, dtype=torch.bfloat16) * 0.05)(D, N) * 3.14),
            "C_real": _w((lambda *s: torch.randn(*s, dtype=torch.bfloat16) * 0.05)(D, N) / N ** 0.5),
            "C_imag": _w((lambda *s: torch.randn(*s, dtype=torch.bfloat16) * 0.05)(D, N) / N ** 0.5),
            "log_dt": _w((lambda *s: torch.randn(*s, dtype=torch.bfloat16) * 0.05)(D) * 0.5 - 4.0),
            "D_skip": _w((lambda *s: torch.randn(*s, dtype=torch.bfloat16) * 0.05)(D)),
        }
    _A_in = _A_setup()

    def _B_setup():
        B, S, D, F = 1, 2048, 640, 1536
        return {
            "x": _w((lambda *s: torch.randn(*s, dtype=torch.bfloat16) * 0.05)(B, S, D)),
            "w1": _w((lambda *s: torch.randn(*s, dtype=torch.bfloat16) * 0.4)(S, F) * (S ** -0.5)),
            "b1": _w((lambda *s: torch.randn(*s, dtype=torch.bfloat16) * 12.8)(F) * 0.02),
            "w2": _w((lambda *s: torch.randn(*s, dtype=torch.bfloat16) * 0.2)(F, S) * (F ** -0.5)),
            "b2": _w((lambda *s: torch.randn(*s, dtype=torch.bfloat16) * 3.2)(S) * 0.02),
        }
    _B_in = _B_setup()

    return {
        "A_u": _A_in["u"],
        "A_log_A_real": _A_in["log_A_real"],
        "A_A_imag": _A_in["A_imag"],
        "A_C_real": _A_in["C_real"],
        "A_C_imag": _A_in["C_imag"],
        "A_log_dt": _A_in["log_dt"],
        "A_D_skip": _A_in["D_skip"],
        "B_x": _B_in["x"],
        "B_w1": _B_in["w1"],
        "B_b1": _B_in["b1"],
        "B_w2": _B_in["w2"],
        "B_b2": _B_in["b2"],
    }

def reference(A_u, A_log_A_real, A_A_imag, A_C_real, A_C_imag, A_log_dt, A_D_skip, B_x, B_w1, B_b1, B_w2, B_b2):
    def _A_ref(u, log_A_real, A_imag, C_real, C_imag, log_dt, D_skip):
        L = u.shape[1]
        dt = torch.exp(log_dt)
        A = -torch.exp(log_A_real) + 1j * A_imag
        C = (C_real + 1j * C_imag) * dt[:, None]
        pos = (lambda x, dt: x.to(dt))(torch.arange(L), log_dt.dtype)
        P = torch.exp((A * dt[:, None])[:, :, None] * pos[None, None, :])
        K = 2.0 * torch.einsum("dn,dnl->dl", C, P).real
        L2 = 2 * L
        u_t = (lambda x, *d: x.permute(*d))(u, 0, 2, 1)
        Kf = (lambda x, *a, **k: torch.fft.rfft(x.float(), *a, **k))(K, L2)
        Uf = (lambda x, *a, **k: torch.fft.rfft(x.float(), *a, **k))(u_t, L2)
        y = (lambda x, *a, **k: torch.fft.irfft(x, *a, **k).to(torch.bfloat16))(Uf * Kf[None, :, :], L2)[:, :, :L]
        y = (lambda x, *d: x.permute(*d))(y, 0, 2, 1)
        return y + u * D_skip[None, None, :]

    def _B_ref(x, w1, b1, w2, b2):
        B, S, D = x.shape
        h = _layernorm(x, axis=-1)
        u = torch.einsum("bsd,sf->bdf", h, w1) + b1
        u = _gelu(u)
        y = torch.einsum("bdf,fs->bds", u, w2) + b2
        return (lambda x, *d: x.permute(*d))(y, 0, 2, 1)

    x = A_u
    h = x + _A_ref(_layernorm(x, axis=-1), A_log_A_real, A_A_imag, A_C_real, A_C_imag, A_log_dt, A_D_skip)
    h2 = h + _B_ref(_layernorm(h, axis=-1), B_w1, B_b1, B_w2, B_b2)
    return h2
```

```cpp
#include <hip/hip_runtime.h>
#include <hip/hip_bf16.h>
#include <stdint.h>

typedef unsigned short u16;
typedef unsigned int u32;
typedef __attribute__((ext_vector_type(8))) short bf16x8;
typedef __attribute__((ext_vector_type(4))) short bf16x4;
typedef __attribute__((ext_vector_type(16))) float f32x16;
typedef __attribute__((ext_vector_type(4))) float f32x4;
typedef __attribute__((ext_vector_type(4))) unsigned int u32x4;

static constexpr int L_ = 2048, D_ = 640, N_ = 192, F_ = 1536;

__device__ __forceinline__ u16 f2b(float f) {
  __hip_bfloat16 b = __float2bfloat16(f);
  return *reinterpret_cast<u16*>(&b);
}
__device__ __forceinline__ float b2f(u16 u) { return __uint_as_float(((u32)u) << 16); }
__device__ __forceinline__ float bfr(float f) { return b2f(f2b(f)); }
__device__ __forceinline__ u32 pack2(float lo, float hi) { return (u32)f2b(lo) | ((u32)f2b(hi) << 16); }
__device__ __forceinline__ float lo_f(u32 w) { return __uint_as_float(w << 16); }
__device__ __forceinline__ float hi_f(u32 w) { return __uint_as_float(w & 0xffff0000u); }

struct c32 { float x, y; };
__device__ __forceinline__ c32 cmul(c32 a, c32 b) { return {a.x * b.x - a.y * b.y, a.x * b.y + a.y * b.x}; }
__device__ __forceinline__ c32 csq(c32 a) { return {a.x * a.x - a.y * a.y, 2.f * a.x * a.y}; }

static constexpr int KA_LN_BLOCKS = 128;
static constexpr int KG_STRIDE = 196;
static constexpr int KG_AROWS = 50, KG_BROWS = 64;

__global__ __launch_bounds__(256) void kA(const u16* __restrict__ x, const u16* __restrict__ lar,
                                          const u16* __restrict__ aim, const u16* __restrict__ cr,
                                          const u16* __restrict__ ci, const u16* __restrict__ ldt,
                                          u16* __restrict__ uT, u16* __restrict__ xT, u16* __restrict__ Kb) {
  __shared__ __attribute__((aligned(16))) u32 sm[(KG_AROWS + KG_BROWS) * (KG_STRIDE / 2 + 2) + 704];
  const int t = threadIdx.x;
  if (blockIdx.x < KA_LN_BLOCKS) {
    const int l0 = blockIdx.x * 16;
    const int r = t >> 4, seg = t & 15;
    u32x4 v[5];
#pragma unroll
    for (int i = 0; i < 5; ++i)
      v[i] = *reinterpret_cast<const u32x4*>(x + (size_t)(l0 + r) * D_ + seg * 8 + 128 * i);
    float s = 0.f;
#pragma unroll
    for (int i = 0; i < 5; ++i)
#pragma unroll
      for (int j = 0; j < 4; ++j) s += lo_f(v[i][j]) + hi_f(v[i][j]);
#pragma unroll
    for (int o = 1; o < 16; o <<= 1) s += __shfl_xor(s, o, 64);
    const float mean = s * (1.f / D_);
    float q = 0.f;
#pragma unroll
    for (int i = 0; i < 5; ++i)
#pragma unroll
      for (int j = 0; j < 4; ++j) {
        float a = lo_f(v[i][j]) - mean, b = hi_f(v[i][j]) - mean;
        q += a * a + b * b;
      }
#pragma unroll
    for (int o = 1; o < 16; o <<= 1) q += __shfl_xor(q, o, 64);
    const float mb = bfr(mean);
    const float den = bfr(sqrtf(bfr(bfr(q * (1.f / D_)) + 1e-5f)));
    u16* T = reinterpret_cast<u16*>(sm);
    for (int pass = 0; pass < 2; ++pass) {
#pragma unroll
      for (int i = 0; i < 5; ++i)
#pragma unroll
        for (int j = 0; j < 4; ++j) {
          const int c = seg * 8 + 128 * i + 2 * j;
          u16 e0, e1;
          if (pass == 0) {
            e0 = f2b(bfr(lo_f(v[i][j]) - mb) / den);
            e1 = f2b(bfr(hi_f(v[i][j]) - mb) / den);
          } else {
            e0 = (u16)(v[i][j] & 0xffffu);
            e1 = (u16)(v[i][j] >> 16);
          }
          T[c * 24 + r] = e0;
          T[(c + 1) * 24 + r] = e1;
        }
      __syncthreads();
      u16* dst = pass == 0 ? uT : xT;
      for (int idx = t; idx < D_ * 2; idx += 256) {
        const int c = idx >> 1, hh = idx & 1;
        const u32x4 w = *reinterpret_cast<const u32x4*>(T + c * 24 + hh * 8);
        *reinterpret_cast<u32x4*>(dst + (size_t)c * L_ + l0 + hh * 8) = w;
      }
      __syncthreads();
    }
    return;
  }
  const int d = blockIdx.x - KA_LN_BLOCKS;
  u32* Al = sm;
  u32* Bl = sm + KG_AROWS * (KG_STRIDE / 2 + 2);
  float* Kp = reinterpret_cast<float*>(sm + (KG_AROWS + KG_BROWS) * (KG_STRIDE / 2 + 2));
  constexpr int RS = KG_STRIDE / 2 + 2;
  u32 ea[41], eb[64];
  if (t < N_) {
    const int gi = d * N_ + t;
    const float dt = bfr(expf(b2f(ldt[d])));
    const float are = -bfr(expf(b2f(lar[gi])));
    const float dre = are * dt, dim = b2f(aim[gi]) * dt;
    float sn, cs;
    sincosf(dim, &sn, &cs);
    const float er = expf(dre);
    c32 z1 = {er * cs, er * sn};
    c32 C = {2.f * b2f(cr[gi]) * dt, 2.f * b2f(ci[gi]) * dt};
    c32 z2 = csq(z1), z4 = csq(z2), z8 = csq(z4), z16 = csq(z8), z32 = csq(z16), z64 = csq(z32), z128 = csq(z64),
        z256 = csq(z128), z512 = csq(z256), z1024 = csq(z512);
    {
      c32 base[4] = {z1, z2, z4, z8};
#pragma unroll
      for (int sg = 0; sg < 4; ++sg) {
        c32 w = {1.f, 0.f};
#pragma unroll
        for (int b = 0; b < 16; ++b) {
          eb[sg * 16 + b] = pack2(w.x, w.y);
          if (b < 15) w = cmul(w, base[sg]);
        }
      }
    }
    {
      c32 w = C;
#pragma unroll
      for (int a = 0; a < 16; ++a) { ea[a] = pack2(w.x, -w.y); w = cmul(w, z16); }
      w = cmul(C, z256);
#pragma unroll
      for (int a = 0; a < 8; ++a) { ea[16 + a] = pack2(w.x, -w.y); w = cmul(w, z32); }
      w = cmul(C, z512);
#pragma unroll
      for (int a = 0; a < 8; ++a) { ea[24 + a] = pack2(w.x, -w.y); w = cmul(w, z64); }
      w = cmul(C, z1024);
#pragma unroll
      for (int a = 0; a < 9; ++a) { ea[32 + a] = pack2(w.x, -w.y); w = cmul(w, z128); }
    }
  }
  const int wv = t >> 6, lane = t & 63;
  f32x4 acc = {0.f, 0.f, 0.f, 0.f};
  const int arow0 = (wv == 0) ? 0 : (16 + (wv - 1) * 9);
  for (int ph = 0; ph < 2; ++ph) {
    if (t < N_ && (t / 96) == ph) {
      const int nn = t % 96;
#pragma unroll
      for (int a = 0; a < 16; ++a) Al[a * RS + nn] = ea[a];
#pragma unroll
      for (int a = 0; a < 8; ++a) Al[(16 + a) * RS + nn] = ea[16 + a];
#pragma unroll
      for (int a = 0; a < 8; ++a) Al[(25 + a) * RS + nn] = ea[24 + a];
#pragma unroll
      for (int a = 0; a < 9; ++a) Al[(34 + a) * RS + nn] = ea[32 + a];
#pragma unroll
      for (int b = 0; b < 64; ++b) Bl[b * RS + nn] = eb[b];
    }
    __syncthreads();
#pragma unroll
    for (int ks = 0; ks < 6; ++ks) {
      const bf16x8 af = *reinterpret_cast<const bf16x8*>(Al + (arow0 + (lane & 15)) * RS + ks * 16 + (lane >> 4) * 4);
      const bf16x8 bfg = *reinterpret_cast<const bf16x8*>(Bl + (wv * 16 + (lane & 15)) * RS + ks * 16 + (lane >> 4) * 4);
      acc = __builtin_amdgcn_mfma_f32_16x16x32_bf16(af, bfg, acc, 0, 0, 0);
    }
    __syncthreads();
  }
  {
    const int b = lane & 15;
#pragma unroll
    for (int rg = 0; rg < 4; ++rg) {
      const int a = (lane >> 4) * 4 + rg;
      if (wv == 0) Kp[16 * a + b] = acc[rg];
      else if (a < 8 || (wv == 3 && a == 8 && b == 0)) Kp[256 + (wv - 1) * 128 + 16 * a + b] = acc[rg];
    }
  }
  __syncthreads();
  {
    u32 o[4];
#pragma unroll
    for (int j = 0; j < 4; ++j) {
      float kv[2];
#pragma unroll
      for (int e = 0; e < 2; ++e) {
        const int l = 8 * t + 2 * j + e;
        const int p = (int)bfr((float)l);
        int idx;
        if (p < 256) idx = p;
        else if (p < 512) idx = 256 + ((p - 256) >> 1);
        else if (p < 1024) idx = 384 + ((p - 512) >> 2);
        else idx = 512 + ((p - 1024) >> 3);
        kv[e] = Kp[idx];
      }
      o[j] = pack2(kv[0], kv[1]);
    }
    u32x4 w = {o[0], o[1], o[2], o[3]};
    *reinterpret_cast<u32x4*>(Kb + (size_t)d * L_ + 8 * t) = w;
  }
}

static constexpr int KB_CS = 4608;
static constexpr int KB_UOFF = 8 * KB_CS;
static constexpr int KB_USTR = 144;
static constexpr int KB_LDS = KB_UOFF + 33 * KB_USTR;

__global__ __launch_bounds__(256) void kB(const u16* __restrict__ uT, const u16* __restrict__ xT,
                                          const u16* __restrict__ Kb, const u16* __restrict__ dsk,
                                          u16* __restrict__ hT) {
  __shared__ __attribute__((aligned(16))) char sm[KB_LDS];
  const int d = blockIdx.x, t = threadIdx.x, wv = t >> 6, lane = t & 63;
  auto cbase = [](int c) -> int {
    const int o = (c == 0) ? 0 : (c == 1) ? 1 : (c == 2 || c == 5) ? 5 : (c == 3 || c == 6) ? 9 : 13;
    return c * KB_CS + 16 * o;
  };
  const u32x4 uv = *reinterpret_cast<const u32x4*>(uT + (size_t)d * L_ + 8 * t);
  *reinterpret_cast<u32x4*>(sm + KB_UOFF + KB_USTR * (1 + (t >> 3)) + (t & 7) * 16) = uv;
  if (t < 9) {
    u32x4 zz = {0u, 0u, 0u, 0u};
    *reinterpret_cast<u32x4*>(sm + KB_UOFF + t * 16) = zz;
  }
  for (int blk = t; blk < 264; blk += 256) {
    u32x4 c1 = {0u, 0u, 0u, 0u}, c2 = {0u, 0u, 0u, 0u};
    u32 e0 = 0u;
    const int i1 = 2040 - 8 * blk, i2 = 2032 - 8 * blk;
    if (i1 >= 0) c1 = *reinterpret_cast<const u32x4*>(Kb + (size_t)d * L_ + i1);
    if (i2 >= 0) c2 = *reinterpret_cast<const u32x4*>(Kb + (size_t)d * L_ + i2);
    if (blk > 0 && blk <= 256) e0 = (u32)Kb[(size_t)d * L_ + 2048 - 8 * blk];
    u32 w[8];
    w[0] = (e0 & 0xffffu) | (c1[3] & 0xffff0000u);
    w[1] = (c1[3] & 0xffffu) | (c1[2] & 0xffff0000u);
    w[2] = (c1[2] & 0xffffu) | (c1[1] & 0xffff0000u);
    w[3] = (c1[1] & 0xffffu) | (c1[0] & 0xffff0000u);
    w[4] = (c1[0] & 0xffffu) | (c2[3] & 0xffff0000u);
    w[5] = (c2[3] & 0xffffu) | (c2[2] & 0xffff0000u);
    w[6] = (c2[2] & 0xffffu) | (c2[1] & 0xffff0000u);
    w[7] = (c2[1] & 0xffffu) | (c2[0] & 0xffff0000u);
#pragma unroll
    for (int c = 0; c < 8; ++c) {
      u32x4 o;
      const int e = c >> 1;
      if ((c & 1) == 0) {
        o[0] = w[e]; o[1] = w[e + 1]; o[2] = w[e + 2]; o[3] = w[e + 3];
      } else {
#pragma unroll
        for (int i = 0; i < 4; ++i) o[i] = __builtin_amdgcn_alignbit(w[e + i + 1], w[e + i], 16);
      }
      *reinterpret_cast<u32x4*>(sm + cbase(c) + blk * 16) = o;
    }
  }
  __syncthreads();
  const int r = lane & 31, h = lane >> 5;
  const int cc = (8 - (r & 7)) & 7, g8 = (r + 7) & ~7;
  const int abase = cbase(cc) + 2 * (2048 + 8 * h - g8) - 64;
  f32x16 acc0 = {}, acc1 = {};
#pragma unroll 1
  for (int dl = wv; dl < 32; dl += 4) {
    const char* ap = sm + abase - 128 * dl;
    int ch = r - dl;
    ch = ch < -1 ? -1 : ch;
    const char* bp = sm + KB_UOFF + KB_USTR * (ch + 1) + 16 * h;
#pragma unroll
    for (int kk = 0; kk < 4; ++kk) {
      const bf16x8 bfr_ = *reinterpret_cast<const bf16x8*>(bp + 32 * kk);
      const bf16x8 a1 = *reinterpret_cast<const bf16x8*>(ap + 32 * kk);
      const bf16x8 a0 = *reinterpret_cast<const bf16x8*>(ap + 64 + 32 * kk);
      acc0 = __builtin_amdgcn_mfma_f32_32x32x16_bf16(a0, bfr_, acc0, 0, 0, 0);
      acc1 = __builtin_amdgcn_mfma_f32_32x32x16_bf16(a1, bfr_, acc1, 0, 0, 0);
    }
  }
  __syncthreads();
  float* red = reinterpret_cast<float*>(sm);
  constexpr int RST = 33;
#pragma unroll
  for (int rg = 0; rg < 16; ++rg) {
    const int row = (rg & 3) + 8 * (rg >> 2) + 4 * h;
    red[(wv * 64 + row) * RST + r] = acc0[rg];
    red[(wv * 64 + 32 + row) * RST + r] = acc1[rg];
  }
  __syncthreads();
  {
    const int a = t >> 3, b0 = (t & 7) * 8;
    const u32x4 xv = *reinterpret_cast<const u32x4*>(xT + (size_t)d * L_ + 8 * t);
    const float ds = b2f(dsk[d]);
    u32 o[4];
#pragma unroll
    for (int j = 0; j < 4; ++j) {
      float hv[2];
#pragma unroll
      for (int e = 0; e < 2; ++e) {
        const int b = b0 + 2 * j + e;
        float y = red[(0 * 64 + b) * RST + a] + red[(1 * 64 + b) * RST + a] + red[(2 * 64 + b) * RST + a] +
                  red[(3 * 64 + b) * RST + a];
        const float uu = e ? hi_f(uv[j]) : lo_f(uv[j]);
        const float xx = e ? hi_f(xv[j]) : lo_f(xv[j]);
        const float r2 = bfr(bfr(y) + bfr(uu * ds));
        hv[e] = xx + r2;
      }
      o[j] = pack2(hv[0], hv[1]);
    }
    u32x4 w = {o[0], o[1], o[2], o[3]};
    *reinterpret_cast<u32x4*>(hT + (size_t)d * L_ + 8 * t) = w;
  }
}

__global__ __launch_bounds__(256) void kC(const u16* __restrict__ hT, u16* __restrict__ hnT) {
  __shared__ float red[4][16];
  const int l0 = blockIdx.x * 16, t = threadIdx.x, hf = t & 1, dr = t >> 1, wv = t >> 6;
  float v[5][8];
#pragma unroll
  for (int i = 0; i < 5; ++i) {
    const u32x4 w = *reinterpret_cast<const u32x4*>(hT + (size_t)(dr + 128 * i) * L_ + l0 + hf * 8);
#pragma unroll
    for (int j = 0; j < 4; ++j) { v[i][2 * j] = lo_f(w[j]); v[i][2 * j + 1] = hi_f(w[j]); }
  }
#pragma unroll
  for (int pass = 0; pass < 2; ++pass) {
    float s[8];
#pragma unroll
    for (int j = 0; j < 8; ++j) {
      s[j] = v[0][j] + v[1][j] + v[2][j] + v[3][j] + v[4][j];
#pragma unroll
      for (int o = 2; o < 64; o <<= 1) s[j] += __shfl_xor(s[j], o, 64);
    }
    if ((t & 63) < 2)
#pragma unroll
      for (int j = 0; j < 8; ++j) red[wv][hf * 8 + j] = s[j];
    __syncthreads();
    float mean[8];
#pragma unroll
    for (int j = 0; j < 8; ++j)
      mean[j] = (red[0][hf * 8 + j] + red[1][hf * 8 + j] + red[2][hf * 8 + j] + red[3][hf * 8 + j]) * (1.f / D_);
    __syncthreads();
#pragma unroll
    for (int j = 0; j < 8; ++j) {
      float q = 0.f;
#pragma unroll
      for (int i = 0; i < 5; ++i) { const float dd = v[i][j] - mean[j]; q += dd * dd; }
#pragma unroll
      for (int o = 2; o < 64; o <<= 1) q += __shfl_xor(q, o, 64);
      s[j] = q;
    }
    if ((t & 63) < 2)
#pragma unroll
      for (int j = 0; j < 8; ++j) red[wv][hf * 8 + j] = s[j];
    __syncthreads();
#pragma unroll
    for (int j = 0; j < 8; ++j) {
      const float var = (red[0][hf * 8 + j] + red[1][hf * 8 + j] + red[2][hf * 8 + j] + red[3][hf * 8 + j]) * (1.f / D_);
      const float mb = bfr(mean[j]);
      const float den = bfr(sqrtf(bfr(bfr(var) + 1e-5f)));
#pragma unroll
      for (int i = 0; i < 5; ++i) v[i][j] = bfr(bfr(v[i][j] - mb) / den);
    }
    __syncthreads();
  }
#pragma unroll
  for (int i = 0; i < 5; ++i) {
    u32x4 w;
#pragma unroll
    for (int j = 0; j < 4; ++j) w[j] = pack2(v[i][2 * j], v[i][2 * j + 1]);
    *reinterpret_cast<u32x4*>(hnT + (size_t)(dr + 128 * i) * L_ + l0 + hf * 8) = w;
  }
}

__device__ __forceinline__ void glds16(const void* g, void* l) {
  __builtin_amdgcn_global_load_lds(reinterpret_cast<const u32*>(g), reinterpret_cast<u32*>(l), 16, 0, 0);
}
__device__ __forceinline__ bf16x4 tr_read(const char* p) {
  bf16x4 r;
  const u32 a = (u32)(size_t)p;
  asm volatile("ds_read_b64_tr_b16 %0, %1" : "=v"(r) : "v"(a) : "memory");
  return r;
}
#define WAIT_LGKM0() asm volatile("s_waitcnt lgkmcnt(0)" ::: "memory")
__device__ __forceinline__ int swzA(int row) { return (row >> 1) & 7; }
__device__ __forceinline__ int swzB(int row) { return (((row >> 1) & 1) << 2) | (((row >> 3) & 1) << 1); }

__device__ __forceinline__ float gelu_tanh(float x) {
  const float y = 0.7978845608028654f * (x + 0.044715f * x * x * x);
  const float e = __expf(2.f * y);
  const float th = 1.f - 2.f / (e + 1.f);
  return 0.5f * x * (1.f + th);
}

static constexpr int D_NST = 6;
__global__ __launch_bounds__(256) void kD(const u16* __restrict__ hnT, const u16* __restrict__ w1,
                                          const u16* __restrict__ b1, u16* __restrict__ G) {
  __shared__ __attribute__((aligned(16))) char sm[D_NST * 16384];
  const int bid = blockIdx.x;
  const int xcd = bid & 7, q = bid >> 3;
  const int ft = xcd * 3 + q / 10, dtile = q % 10;
  const int d0 = dtile * 64, f0 = ft * 64;
  const int t = threadIdx.x, wv = t >> 6, lane = t & 63;
  const int wr = wv >> 1, wc = wv & 1;
  const u16* asrc[2];
  const u16* bsrc[2];
#pragma unroll
  for (int i = 0; i < 2; ++i) {
    const int slot = (wv * 2 + i) * 64 + lane;
    const int row = slot >> 3, chs = slot & 7;
    asrc[i] = hnT + (size_t)(d0 + row) * L_ + 8 * (chs ^ swzA(row));
    bsrc[i] = w1 + (size_t)row * F_ + f0 + 8 * (chs ^ swzB(row));
  }
  auto issue = [&](int kt) {
    char* st = sm + (kt % D_NST) * 16384;
#pragma unroll
    for (int i = 0; i < 2; ++i) {
      glds16(asrc[i] + kt * 64, st + (wv * 2 + i) * 1024);
      glds16(bsrc[i] + (size_t)kt * 64 * F_, st + 8192 + (wv * 2 + i) * 1024);
    }
  };
  constexpr int NK = L_ / 64;
#pragma unroll
  for (int s = 0; s < D_NST - 1; ++s) issue(s);
  f32x16 acc = {};
  const int r = lane & 31, h = lane >> 5;
  const int arow = wr * 32 + r;
  const int bq = (lane & 15) >> 2, bp_ = lane & 3, bblk = (lane >> 4) & 1;
#pragma unroll 1
  for (int kt = 0; kt < NK; ++kt) {
    if (kt + D_NST - 1 < NK) {
      asm volatile("s_waitcnt vmcnt(%0)" ::"n"(4 * (D_NST - 2)) : "memory");
    } else {
      asm volatile("s_waitcnt vmcnt(0)" ::: "memory");
    }
    __builtin_amdgcn_s_barrier();
    if (kt + D_NST - 1 < NK) issue(kt + D_NST - 1);
    const char* sa = sm + (kt % D_NST) * 16384;
    const char* sb = sa + 8192;
#pragma unroll
    for (int ks = 0; ks < 4; ++ks) {
      const bf16x8 af = *reinterpret_cast<const bf16x8*>(sa + arow * 128 + 16 * ((2 * ks + h) ^ swzA(arow)));
      bf16x8 bfg;
#pragma unroll
      for (int tt = 0; tt < 2; ++tt) {
        const int krow = 16 * ks + 8 * h + 4 * tt + bq;
        const int chunk = 4 * wc + 2 * bblk + (bp_ >> 1);
        const bf16x4 x4 = tr_read(sb + krow * 128 + 16 * (chunk ^ swzB(krow)) + 8 * (bp_ & 1));
        bfg[4 * tt + 0] = x4[0]; bfg[4 * tt + 1] = x4[1]; bfg[4 * tt + 2] = x4[2]; bfg[4 * tt + 3] = x4[3];
      }
      WAIT_LGKM0();
      acc = __builtin_amdgcn_mfma_f32_32x32x16_bf16(af, bfg, acc, 0, 0, 0);
    }
  }
  const int f = f0 + wc * 32 + r;
  const float bias = b2f(b1[f]);
#pragma unroll
  for (int rg = 0; rg < 16; ++rg) {
    const int dd = d0 + wr * 32 + (rg & 3) + 8 * (rg >> 2) + 4 * h;
    const float v = bfr(bfr(acc[rg]) + bias);
    G[(size_t)dd * F_ + f] = f2b(gelu_tanh(v));
  }
}

static constexpr int E_NST = 6;
static constexpr int E_STAGE = 8192 + 10240;
__global__ __launch_bounds__(256) void kE(const u16* __restrict__ G, const u16* __restrict__ w2,
                                          const u16* __restrict__ b2, const u16* __restrict__ hT,
                                          u16* __restrict__ out) {
  __shared__ __attribute__((aligned(16))) char sm[E_NST * E_STAGE];
  const int bid = blockIdx.x;
  const int xcd = bid & 7, q = bid >> 3;
  const int stile = xcd * 4 + (q >> 3), dtile = q & 7;
  const int s0 = stile * 64, d0 = dtile * 80;
  const int t = threadIdx.x, wv = t >> 6, lane = t & 63;
  const u16* asrc[2];
  const u16* bsrc[3];
#pragma unroll
  for (int i = 0; i < 2; ++i) {
    const int slot = (wv * 2 + i) * 64 + lane;
    const int row = slot >> 3, chs = slot & 7;
    asrc[i] = w2 + (size_t)row * L_ + s0 + 8 * (chs ^ swzB(row));
  }
  const int nb = (wv < 2) ? 3 : 2;
  const int bpiece0 = (wv < 2) ? wv * 3 : 6 + (wv - 2) * 2;
#pragma unroll
  for (int i = 0; i < 3; ++i) {
    const int slot = (bpiece0 + i) * 64 + lane;
    const int row = (slot >> 3) % 80, chs = slot & 7;
    bsrc[i] = G + (size_t)(d0 + row) * F_ + 8 * (chs ^ swzA(row));
  }
  auto issue = [&](int kt) {
    char* st = sm + (kt % E_NST) * E_STAGE;
#pragma unroll
    for (int i = 0; i < 2; ++i) glds16(asrc[i] + (size_t)kt * 64 * L_, st + (wv * 2 + i) * 1024);
#pragma unroll
    for (int i = 0; i < 3; ++i)
      if (i < nb) glds16(bsrc[i] + kt * 64, st + 8192 + (bpiece0 + i) * 1024);
  };
  constexpr int NK = F_ / 64;
#pragma unroll
  for (int s = 0; s < E_NST - 1; ++s) issue(s);
  f32x4 acc[5];
#pragma unroll
  for (int i = 0; i < 5; ++i) acc[i] = (f32x4){0.f, 0.f, 0.f, 0.f};
  const int g = lane >> 4, aq = (lane & 15) >> 2, ap_ = lane & 3;
#pragma unroll 1
  for (int kt = 0; kt < NK; ++kt) {
    if (kt + E_NST - 1 < NK) {
      if (wv < 2) asm volatile("s_waitcnt vmcnt(%0)" ::"n"(5 * (E_NST - 2)) : "memory");
      else asm volatile("s_waitcnt vmcnt(%0)" ::"n"(4 * (E_NST - 2)) : "memory");
    } else {
      asm volatile("s_waitcnt vmcnt(0)" ::: "memory");
    }
    __builtin_amdgcn_s_barrier();
    if (kt + E_NST - 1 < NK) issue(kt + E_NST - 1);
    const char* sa = sm + (kt % E_NST) * E_STAGE;
    const char* sb = sa + 8192;
#pragma unroll
    for (int ks = 0; ks < 2; ++ks) {
      bf16x8 af;
#pragma unroll
      for (int tt = 0; tt < 2; ++tt) {
        const int krow = 32 * ks + 8 * g + 4 * tt + aq;
        const int chunk = 2 * wv + (ap_ >> 1);
        const bf16x4 x4 = tr_read(sa + krow * 128 + 16 * (chunk ^ swzB(krow)) + 8 * (ap_ & 1));
        af[4 * tt + 0] = x4[0]; af[4 * tt + 1] = x4[1]; af[4 * tt + 2] = x4[2]; af[4 * tt + 3] = x4[3];
      }
      bf16x8 bfg[5];
#pragma unroll
      for (int n = 0; n < 5; ++n) {
        const int brow = n * 16 + (lane & 15);
        bfg[n] = *reinterpret_cast<const bf16x8*>(sb + brow * 128 + 16 * ((4 * ks + g) ^ swzA(brow)));
      }
      WAIT_LGKM0();
#pragma unroll
      for (int n = 0; n < 5; ++n) acc[n] = __builtin_amdgcn_mfma_f32_16x16x32_bf16(af, bfg[n], acc[n], 0, 0, 0);
    }
  }
  const int sr = s0 + wv * 16 + g * 4;
  float bias[4];
#pragma unroll
  for (int rg = 0; rg < 4; ++rg) bias[rg] = b2f(b2[sr + rg]);
#pragma unroll
  for (int n = 0; n < 5; ++n) {
    const int dd = d0 + n * 16 + (lane & 15);
    const uint2 hv = *reinterpret_cast<const uint2*>(hT + (size_t)dd * L_ + sr);
    const float hh[4] = {lo_f(hv.x), hi_f(hv.x), lo_f(hv.y), hi_f(hv.y)};
#pragma unroll
    for (int rg = 0; rg < 4; ++rg) {
      const float v = bfr(bfr(acc[n][rg]) + bias[rg]);
      out[(size_t)(sr + rg) * D_ + dd] = f2b(hh[rg] + v);
    }
  }
}

extern "C" void kernel_launch(void* const* d_in, const int* in_sizes, int n_in, void* d_out, int out_size, void* d_ws,
                              size_t ws_size, hipStream_t stream) {
  const u16* x = (const u16*)d_in[0];
  const u16* lar = (const u16*)d_in[1];
  const u16* aim = (const u16*)d_in[2];
  const u16* cr = (const u16*)d_in[3];
  const u16* ci = (const u16*)d_in[4];
  const u16* ldt = (const u16*)d_in[5];
  const u16* dsk = (const u16*)d_in[6];
  const u16* w1 = (const u16*)d_in[8];
  const u16* b1 = (const u16*)d_in[9];
  const u16* w2 = (const u16*)d_in[10];
  const u16* b2 = (const u16*)d_in[11];
  char* ws = (char*)d_ws;
  const size_t TS = (size_t)L_ * D_ * 2;
  u16* uT = (u16*)(ws + 0 * TS);
  u16* xT = (u16*)(ws + 1 * TS);
  u16* Kb = (u16*)(ws + 2 * TS);
  u16* hT = (u16*)(ws + 3 * TS);
  u16* hnT = (u16*)(ws + 4 * TS);
  u16* G = (u16*)(ws + 5 * TS);
  kA<<<KA_LN_BLOCKS + D_, 256, 0, stream>>>(x, lar, aim, cr, ci, ldt, uT, xT, Kb);
  kB<<<D_, 256, 0, stream>>>(uT, xT, Kb, dsk, hT);
  kC<<<L_ / 16, 256, 0, stream>>>(hT, hnT);
  kD<<<240, 256, 0, stream>>>(hnT, w1, b1, G);
  kE<<<256, 256, 0, stream>>>(G, w2, b2, hT, (u16*)d_out);
}
```

```cpp
#include <hip/hip_runtime.h>
#include <hip/hip_bf16.h>
#include <stdint.h>

typedef unsigned short u16;
typedef unsigned int u32;
typedef __attribute__((ext_vector_type(8))) short bf16x8;
typedef __attribute__((ext_vector_type(4))) short bf16x4;
typedef __attribute__((ext_vector_type(16))) float f32x16;
typedef __attribute__((ext_vector_type(4))) float f32x4;
typedef __attribute__((ext_vector_type(4))) unsigned int u32x4;

static constexpr int L_ = 2048, D_ = 640, N_ = 192, F_ = 1536;

__device__ __forceinline__ u16 f2b(float f) {
  __hip_bfloat16 b = __float2bfloat16(f);
  return *reinterpret_cast<u16*>(&b);
}
__device__ __forceinline__ float b2f(u16 u) { return __uint_as_float(((u32)u) << 16); }
__device__ __forceinline__ float bfr(float f) { return b2f(f2b(f)); }
__device__ __forceinline__ u32 pack2(float lo, float hi) { return (u32)f2b(lo) | ((u32)f2b(hi) << 16); }
__device__ __forceinline__ float lo_f(u32 w) { return __uint_as_float(w << 16); }
__device__ __forceinline__ float hi_f(u32 w) { return __uint_as_float(w & 0xffff0000u); }

#define STAMP(kid,i) do{}while(0)
struct c32 { float x, y; };
__device__ __forceinline__ c32 cmul(c32 a, c32 b) { return {a.x * b.x - a.y * b.y, a.x * b.y + a.y * b.x}; }
__device__ __forceinline__ c32 csq(c32 a) { return {a.x * a.x - a.y * a.y, 2.f * a.x * a.y}; }

static constexpr int KA_LN_BLOCKS = 128;
static constexpr int KG_STRIDE = 196;
static constexpr int KG_AROWS = 50, KG_BROWS = 64;

__global__ __launch_bounds__(256) void kA(const u16* __restrict__ x, const u16* __restrict__ lar,
                                          const u16* __restrict__ aim, const u16* __restrict__ cr,
                                          const u16* __restrict__ ci, const u16* __restrict__ ldt,
                                          u16* __restrict__ uT, u16* __restrict__ xT, u16* __restrict__ Kb) {
  __shared__ __attribute__((aligned(16))) u32 sm[(KG_AROWS + KG_BROWS) * (KG_STRIDE / 2 + 2) + 704];
  const int t = threadIdx.x;
  if (blockIdx.x < KA_LN_BLOCKS) {
    const int l0 = blockIdx.x * 16;
    const int r = t >> 4, seg = t & 15;
    u32x4 v[5];
#pragma unroll
    for (int i = 0; i < 5; ++i)
      v[i] = *reinterpret_cast<const u32x4*>(x + (size_t)(l0 + r) * D_ + seg * 8 + 128 * i);
    float s = 0.f;
#pragma unroll
    for (int i = 0; i < 5; ++i)
#pragma unroll
      for (int j = 0; j < 4; ++j) s += lo_f(v[i][j]) + hi_f(v[i][j]);
#pragma unroll
    for (int o = 1; o < 16; o <<= 1) s += __shfl_xor(s, o, 64);
    const float mean = s * (1.f / D_);
    float q = 0.f;
#pragma unroll
    for (int i = 0; i < 5; ++i)
#pragma unroll
      for (int j = 0; j < 4; ++j) {
        float a = lo_f(v[i][j]) - mean, b = hi_f(v[i][j]) - mean;
        q += a * a + b * b;
      }
#pragma unroll
    for (int o = 1; o < 16; o <<= 1) q += __shfl_xor(q, o, 64);
    const float mb = bfr(mean);
    const float rden = 1.f / bfr(sqrtf(bfr(bfr(q * (1.f / D_)) + 1e-5f)));
    u16* T = reinterpret_cast<u16*>(sm);
    for (int pass = 0; pass < 2; ++pass) {
#pragma unroll
      for (int i = 0; i < 5; ++i)
#pragma unroll
        for (int j = 0; j < 4; ++j) {
          const int c = seg * 8 + 128 * i + 2 * j;
          u16 e0, e1;
          if (pass == 0) {
            e0 = f2b(bfr(lo_f(v[i][j]) - mb) * rden);
            e1 = f2b(bfr(hi_f(v[i][j]) - mb) * rden);
          } else {
            e0 = (u16)(v[i][j] & 0xffffu);
            e1 = (u16)(v[i][j] >> 16);
          }
          T[c * 24 + r] = e0;
          T[(c + 1) * 24 + r] = e1;
        }
      __syncthreads();
      u16* dst = pass == 0 ? uT : xT;
      for (int idx = t; idx < D_ * 2; idx += 256) {
        const int c = idx >> 1, hh = idx & 1;
        const u32x4 w = *reinterpret_cast<const u32x4*>(T + c * 24 + hh * 8);
        *reinterpret_cast<u32x4*>(dst + (size_t)c * L_ + l0 + hh * 8) = w;
      }
      __syncthreads();
    }
    return;
  }
  const int d = blockIdx.x - KA_LN_BLOCKS;
  u32* Al = sm;
  u32* Bl = sm + KG_AROWS * (KG_STRIDE / 2 + 2);
  float* Kp = reinterpret_cast<float*>(sm + (KG_AROWS + KG_BROWS) * (KG_STRIDE / 2 + 2));
  constexpr int RS = KG_STRIDE / 2 + 2;
  STAMP(0,0);
  u32 ea[41], eb[64];
  if (t < N_) {
    const int gi = d * N_ + t;
    const float dt = bfr(expf(b2f(ldt[d])));
    const float are = -bfr(expf(b2f(lar[gi])));
    const float dre = are * dt, dim = b2f(aim[gi]) * dt;
    float sn, cs;
    __sincosf(dim, &sn, &cs);
    const float er = __expf(dre);
    c32 z1 = {er * cs, er * sn};
    c32 C = {2.f * b2f(cr[gi]) * dt, 2.f * b2f(ci[gi]) * dt};
    c32 z2 = csq(z1), z4 = csq(z2), z8 = csq(z4), z16 = csq(z8), z32 = csq(z16), z64 = csq(z32), z128 = csq(z64),
        z256 = csq(z128), z512 = csq(z256), z1024 = csq(z512);
    {
      c32 base[4] = {z1, z2, z4, z8};
#pragma unroll
      for (int sg = 0; sg < 4; ++sg) {
        c32 w = {1.f, 0.f};
#pragma unroll
        for (int b = 0; b < 16; ++b) {
          eb[sg * 16 + b] = pack2(w.x, w.y);
          if (b < 15) w = cmul(w, base[sg]);
        }
      }
    }
    {
      c32 w = C;
#pragma unroll
      for (int a = 0; a < 16; ++a) { ea[a] = pack2(w.x, -w.y); w = cmul(w, z16); }
      w = cmul(C, z256);
#pragma unroll
      for (int a = 0; a < 8; ++a) { ea[16 + a] = pack2(w.x, -w.y); w = cmul(w, z32); }
      w = cmul(C, z512);
#pragma unroll
      for (int a = 0; a < 8; ++a) { ea[24 + a] = pack2(w.x, -w.y); w = cmul(w, z64); }
      w = cmul(C, z1024);
#pragma unroll
      for (int a = 0; a < 9; ++a) { ea[32 + a] = pack2(w.x, -w.y); w = cmul(w, z128); }
    }
  }
  STAMP(0,1);
  const int wv = t >> 6, lane = t & 63;
  f32x4 acc = {0.f, 0.f, 0.f, 0.f};
  const int arow0 = (wv == 0) ? 0 : (16 + (wv - 1) * 9);
  for (int ph = 0; ph < 2; ++ph) {
    if (t < N_ && (t / 96) == ph) {
      const int nn = t % 96;
#pragma unroll
      for (int a = 0; a < 16; ++a) Al[a * RS + nn] = ea[a];
#pragma unroll
      for (int a = 0; a < 8; ++a) Al[(16 + a) * RS + nn] = ea[16 + a];
#pragma unroll
      for (int a = 0; a < 8; ++a) Al[(25 + a) * RS + nn] = ea[24 + a];
#pragma unroll
      for (int a = 0; a < 9; ++a) Al[(34 + a) * RS + nn] = ea[32 + a];
#pragma unroll
      for (int b = 0; b < 64; ++b) Bl[b * RS + nn] = eb[b];
    }
    __syncthreads();
#pragma unroll
    for (int ks = 0; ks < 6; ++ks) {
      const bf16x8 af = *reinterpret_cast<const bf16x8*>(Al + (arow0 + (lane & 15)) * RS + ks * 16 + (lane >> 4) * 4);
      const bf16x8 bfg = *reinterpret_cast<const bf16x8*>(Bl + (wv * 16 + (lane & 15)) * RS + ks * 16 + (lane >> 4) * 4);
      acc = __builtin_amdgcn_mfma_f32_16x16x32_bf16(af, bfg, acc, 0, 0, 0);
    }
    __syncthreads();
  }
  STAMP(0,2);
  {
    const int b = lane & 15;
#pragma unroll
    for (int rg = 0; rg < 4; ++rg) {
      const int a = (lane >> 4) * 4 + rg;
      if (wv == 0) Kp[16 * a + b] = acc[rg];
      else if (a < 8 || (wv == 3 && a == 8 && b == 0)) Kp[256 + (wv - 1) * 128 + 16 * a + b] = acc[rg];
    }
  }
  __syncthreads();
  {
    u32 o[4];
#pragma unroll
    for (int j = 0; j < 4; ++j) {
      float kv[2];
#pragma unroll
      for (int e = 0; e < 2; ++e) {
        const int l = 8 * t + 2 * j + e;
        const int p = (int)bfr((float)l);
        int idx;
        if (p < 256) idx = p;
        else if (p < 512) idx = 256 + ((p - 256) >> 1);
        else if (p < 1024) idx = 384 + ((p - 512) >> 2);
        else idx = 512 + ((p - 1024) >> 3);
        kv[e] = Kp[idx];
      }
      o[j] = pack2(kv[0], kv[1]);
    }
    u32x4 w = {o[0], o[1], o[2], o[3]};
    *reinterpret_cast<u32x4*>(Kb + (size_t)d * L_ + 8 * t) = w;
  }
  STAMP(0,3);
}

static constexpr int KB_CS = 4608;
static constexpr int KB_UOFF = 8 * KB_CS;
static constexpr int KB_USTR = 144;
static constexpr int KB_LDS = KB_UOFF + 33 * KB_USTR;

__global__ __launch_bounds__(256) void kB(const u16* __restrict__ uT, const u16* __restrict__ xT,
                                          const u16* __restrict__ Kb, const u16* __restrict__ dsk,
                                          u16* __restrict__ hT) {
  __shared__ __attribute__((aligned(16))) char sm[KB_LDS];
  const int d = blockIdx.x, t = threadIdx.x, wv = t >> 6, lane = t & 63;
  auto cbase = [](int c) -> int {
    const int o = (c == 0) ? 0 : (c == 1) ? 1 : (c == 2 || c == 5) ? 5 : (c == 3 || c == 6) ? 9 : 13;
    return c * KB_CS + 16 * o;
  };
  STAMP(1,0);
  const u32x4 uv = *reinterpret_cast<const u32x4*>(uT + (size_t)d * L_ + 8 * t);
  *reinterpret_cast<u32x4*>(sm + KB_UOFF + KB_USTR * (1 + (t >> 3)) + (t & 7) * 16) = uv;
  if (t < 9) {
    u32x4 zz = {0u, 0u, 0u, 0u};
    *reinterpret_cast<u32x4*>(sm + KB_UOFF + t * 16) = zz;
  }
  for (int blk = t; blk < 264; blk += 256) {
    u32x4 c1 = {0u, 0u, 0u, 0u}, c2 = {0u, 0u, 0u, 0u};
    u32 e0 = 0u;
    const int i1 = 2040 - 8 * blk, i2 = 2032 - 8 * blk;
    if (i1 >= 0) c1 = *reinterpret_cast<const u32x4*>(Kb + (size_t)d * L_ + i1);
    if (i2 >= 0) c2 = *reinterpret_cast<const u32x4*>(Kb + (size_t)d * L_ + i2);
    if (blk > 0 && blk <= 256) e0 = (u32)Kb[(size_t)d * L_ + 2048 - 8 * blk];
    u32 w[8];
    w[0] = (e0 & 0xffffu) | (c1[3] & 0xffff0000u);
    w[1] = (c1[3] & 0xffffu) | (c1[2] & 0xffff0000u);
    w[2] = (c1[2] & 0xffffu) | (c1[1] & 0xffff0000u);
    w[3] = (c1[1] & 0xffffu) | (c1[0] & 0xffff0000u);
    w[4] = (c1[0] & 0xffffu) | (c2[3] & 0xffff0000u);
    w[5] = (c2[3] & 0xffffu) | (c2[2] & 0xffff0000u);
    w[6] = (c2[2] & 0xffffu) | (c2[1] & 0xffff0000u);
    w[7] = (c2[1] & 0xffffu) | (c2[0] & 0xffff0000u);
#pragma unroll
    for (int c = 0; c < 8; ++c) {
      u32x4 o;
      const int e = c >> 1;
      if ((c & 1) == 0) {
        o[0] = w[e]; o[1] = w[e + 1]; o[2] = w[e + 2]; o[3] = w[e + 3];
      } else {
#pragma unroll
        for (int i = 0; i < 4; ++i) o[i] = __builtin_amdgcn_alignbit(w[e + i + 1], w[e + i], 16);
      }
      *reinterpret_cast<u32x4*>(sm + cbase(c) + blk * 16) = o;
    }
  }
  STAMP(1,1);
  __syncthreads();
  STAMP(1,2);
  const int r = lane & 31, h = lane >> 5;
  const int cc = (8 - (r & 7)) & 7, g8 = (r + 7) & ~7;
  const int abase = cbase(cc) + 2 * (2048 + 8 * h - g8) - 64;
  f32x16 acc0 = {}, acc1 = {};
#pragma unroll 1
  for (int dl = wv; dl < 32; dl += 4) {
    const char* ap = sm + abase - 128 * dl;
    int ch = r - dl;
    ch = ch < -1 ? -1 : ch;
    const char* bp = sm + KB_UOFF + KB_USTR * (ch + 1) + 16 * h;
#pragma unroll
    for (int kk = 0; kk < 4; ++kk) {
      const bf16x8 bfr_ = *reinterpret_cast<const bf16x8*>(bp + 32 * kk);
      const bf16x8 a1 = *reinterpret_cast<const bf16x8*>(ap + 32 * kk);
      const bf16x8 a0 = *reinterpret_cast<const bf16x8*>(ap + 64 + 32 * kk);
      acc0 = __builtin_amdgcn_mfma_f32_32x32x16_bf16(a0, bfr_, acc0, 0, 0, 0);
      acc1 = __builtin_amdgcn_mfma_f32_32x32x16_bf16(a1, bfr_, acc1, 0, 0, 0);
    }
  }
  STAMP(1,3);
  __syncthreads();
  float* red = reinterpret_cast<float*>(sm);
  constexpr int RST = 33;
#pragma unroll
  for (int rg = 0; rg < 16; ++rg) {
    const int row = (rg & 3) + 8 * (rg >> 2) + 4 * h;
    red[(wv * 64 + row) * RST + r] = acc0[rg];
    red[(wv * 64 + 32 + row) * RST + r] = acc1[rg];
  }
  __syncthreads();
  {
    const int a = t >> 3, b0 = (t & 7) * 8;
    const u32x4 xv = *reinterpret_cast<const u32x4*>(xT + (size_t)d * L_ + 8 * t);
    const float ds = b2f(dsk[d]);
    u32 o[4];
#pragma unroll
    for (int j = 0; j < 4; ++j) {
      float hv[2];
#pragma unroll
      for (int e = 0; e < 2; ++e) {
        const int b = b0 + 2 * j + e;
        float y = red[(0 * 64 + b) * RST + a] + red[(1 * 64 + b) * RST + a] + red[(2 * 64 + b) * RST + a] +
                  red[(3 * 64 + b) * RST + a];
        const float uu = e ? hi_f(uv[j]) : lo_f(uv[j]);
        const float xx = e ? hi_f(xv[j]) : lo_f(xv[j]);
        const float r2 = bfr(bfr(y) + bfr(uu * ds));
        hv[e] = xx + r2;
      }
      o[j] = pack2(hv[0], hv[1]);
    }
    u32x4 w = {o[0], o[1], o[2], o[3]};
    *reinterpret_cast<u32x4*>(hT + (size_t)d * L_ + 8 * t) = w;
  }
  STAMP(1,4);
}

__global__ __launch_bounds__(256) void kC(const u16* __restrict__ hT, u16* __restrict__ hnT) {
  __shared__ float red[4][16];
  const int l0 = blockIdx.x * 16, t = threadIdx.x, hf = t & 1, dr = t >> 1, wv = t >> 6;
  float v[5][8];
#pragma unroll
  for (int i = 0; i < 5; ++i) {
    const u32x4 w = *reinterpret_cast<const u32x4*>(hT + (size_t)(dr + 128 * i) * L_ + l0 + hf * 8);
#pragma unroll
    for (int j = 0; j < 4; ++j) { v[i][2 * j] = lo_f(w[j]); v[i][2 * j + 1] = hi_f(w[j]); }
  }
#pragma unroll
  for (int pass = 0; pass < 2; ++pass) {
    float s[8];
#pragma unroll
    for (int j = 0; j < 8; ++j) {
      s[j] = v[0][j] + v[1][j] + v[2][j] + v[3][j] + v[4][j];
#pragma unroll
      for (int o = 2; o < 64; o <<= 1) s[j] += __shfl_xor(s[j], o, 64);
    }
    if ((t & 63) < 2)
#pragma unroll
      for (int j = 0; j < 8; ++j) red[wv][hf * 8 + j] = s[j];
    __syncthreads();
    float mean[8];
#pragma unroll
    for (int j = 0; j < 8; ++j)
      mean[j] = (red[0][hf * 8 + j] + red[1][hf * 8 + j] + red[2][hf * 8 + j] + red[3][hf * 8 + j]) * (1.f / D_);
    __syncthreads();
#pragma unroll
    for (int j = 0; j < 8; ++j) {
      float q = 0.f;
#pragma unroll
      for (int i = 0; i < 5; ++i) { const float dd = v[i][j] - mean[j]; q += dd * dd; }
#pragma unroll
      for (int o = 2; o < 64; o <<= 1) q += __shfl_xor(q, o, 64);
      s[j] = q;
    }
    if ((t & 63) < 2)
#pragma unroll
      for (int j = 0; j < 8; ++j) red[wv][hf * 8 + j] = s[j];
    __syncthreads();
#pragma unroll
    for (int j = 0; j < 8; ++j) {
      const float var = (red[0][hf * 8 + j] + red[1][hf * 8 + j] + red[2][hf * 8 + j] + red[3][hf * 8 + j]) * (1.f / D_);
      const float mb = bfr(mean[j]);
      const float rden = 1.f / bfr(sqrtf(bfr(bfr(var) + 1e-5f)));
#pragma unroll
      for (int i = 0; i < 5; ++i) v[i][j] = bfr(bfr(v[i][j] - mb) * rden);
    }
    __syncthreads();
  }
#pragma unroll
  for (int i = 0; i < 5; ++i) {
    u32x4 w;
#pragma unroll
    for (int j = 0; j < 4; ++j) w[j] = pack2(v[i][2 * j], v[i][2 * j + 1]);
    *reinterpret_cast<u32x4*>(hnT + (size_t)(dr + 128 * i) * L_ + l0 + hf * 8) = w;
  }
}

__device__ __forceinline__ void glds16(const void* g, void* l) {
  __builtin_amdgcn_global_load_lds(reinterpret_cast<const u32*>(g), reinterpret_cast<u32*>(l), 16, 0, 0);
}
__device__ __forceinline__ bf16x4 tr_read(const char* p) {
  typedef __attribute__((address_space(3))) bf16x4 lds_v4;
  return __builtin_amdgcn_ds_read_tr16_b64_v4i16((lds_v4*)(u32)(size_t)p);
}
#define WAIT_LGKM0() asm volatile("s_waitcnt lgkmcnt(0)" ::: "memory")
__device__ __forceinline__ int swzA(int row) { return (row >> 1) & 7; }
__device__ __forceinline__ int swzB(int row) { return (((row >> 1) & 1) << 2) | (((row >> 3) & 1) << 1); }

__device__ __forceinline__ float gelu_tanh(float x) {
  const float y = 0.7978845608028654f * (x + 0.044715f * x * x * x);
  const float e = __expf(2.f * y);
  const float th = 1.f - 2.f / (e + 1.f);
  return 0.5f * x * (1.f + th);
}

static constexpr int D_NST = 6;
__global__ __launch_bounds__(256) void kD(const u16* __restrict__ hnT, const u16* __restrict__ w1,
                                          const u16* __restrict__ b1, u16* __restrict__ G) {
  __shared__ __attribute__((aligned(16))) char sm[D_NST * 16384];
  const int bid = blockIdx.x;
  const int xcd = bid & 7, q = bid >> 3;
  const int ft = xcd * 3 + q / 10, dtile = q % 10;
  const int d0 = dtile * 64, f0 = ft * 64;
  const int t = threadIdx.x, wv = t >> 6, lane = t & 63;
  const int wr = wv >> 1, wc = wv & 1;
  const u16* asrc[2];
  const u16* bsrc[2];
#pragma unroll
  for (int i = 0; i < 2; ++i) {
    const int slot = (wv * 2 + i) * 64 + lane;
    const int row = slot >> 3, chs = slot & 7;
    asrc[i] = hnT + (size_t)(d0 + row) * L_ + 8 * (chs ^ swzA(row));
    bsrc[i] = w1 + (size_t)row * F_ + f0 + 8 * (chs ^ swzB(row));
  }
  auto issue = [&](int kt) {
    char* st = sm + (kt % D_NST) * 16384;
#pragma unroll
    for (int i = 0; i < 2; ++i) {
      glds16(asrc[i] + kt * 64, st + (wv * 2 + i) * 1024);
      glds16(bsrc[i] + (size_t)kt * 64 * F_, st + 8192 + (wv * 2 + i) * 1024);
    }
  };
  constexpr int NK = L_ / 64;
#pragma unroll
  for (int s = 0; s < D_NST - 1; ++s) issue(s);
  f32x16 acc = {};
  const int r = lane & 31, h = lane >> 5;
  const int arow = wr * 32 + r;
  const int bq = (lane & 15) >> 2, bp_ = lane & 3, bblk = (lane >> 4) & 1;
#pragma unroll 1
  for (int kt = 0; kt < NK; ++kt) {
    if (kt + D_NST - 1 < NK) {
      asm volatile("s_waitcnt vmcnt(%0)" ::"n"(4 * (D_NST - 2)) : "memory");
    } else {
      asm volatile("s_waitcnt vmcnt(0)" ::: "memory");
    }
    __builtin_amdgcn_s_barrier();
    if (kt + D_NST - 1 < NK) issue(kt + D_NST - 1);
    const char* sa = sm + (kt % D_NST) * 16384;
    const char* sb = sa + 8192;
    bf16x8 af[4], bfg[4];
#pragma unroll
    for (int ks = 0; ks < 4; ++ks) {
      af[ks] = *reinterpret_cast<const bf16x8*>(sa + arow * 128 + 16 * ((2 * ks + h) ^ swzA(arow)));
#pragma unroll
      for (int tt = 0; tt < 2; ++tt) {
        const int krow = 16 * ks + 8 * h + 4 * tt + bq;
        const int chunk = 4 * wc + 2 * bblk + (bp_ >> 1);
        const bf16x4 x4 = tr_read(sb + krow * 128 + 16 * (chunk ^ swzB(krow)) + 8 * (bp_ & 1));
        bfg[ks][4 * tt + 0] = x4[0]; bfg[ks][4 * tt + 1] = x4[1]; bfg[ks][4 * tt + 2] = x4[2]; bfg[ks][4 * tt + 3] = x4[3];
      }
    }
    WAIT_LGKM0();
#pragma unroll
    for (int ks = 0; ks < 4; ++ks) acc = __builtin_amdgcn_mfma_f32_32x32x16_bf16(af[ks], bfg[ks], acc, 0, 0, 0);
  }
  const int f = f0 + wc * 32 + r;
  const float bias = b2f(b1[f]);
#pragma unroll
  for (int rg = 0; rg < 16; ++rg) {
    const int dd = d0 + wr * 32 + (rg & 3) + 8 * (rg >> 2) + 4 * h;
    const float v = bfr(bfr(acc[rg]) + bias);
    G[(size_t)dd * F_ + f] = f2b(gelu_tanh(v));
  }
}

static constexpr int E_NST = 6;
static constexpr int E_STAGE = 8192 + 10240;
__global__ __launch_bounds__(256) void kE(const u16* __restrict__ G, const u16* __restrict__ w2,
                                          const u16* __restrict__ b2, const u16* __restrict__ hT,
                                          u16* __restrict__ out) {
  __shared__ __attribute__((aligned(16))) char sm[E_NST * E_STAGE];
  const int bid = blockIdx.x;
  const int xcd = bid & 7, q = bid >> 3;
  const int stile = xcd * 4 + (q >> 3), dtile = q & 7;
  const int s0 = stile * 64, d0 = dtile * 80;
  const int t = threadIdx.x, wv = t >> 6, lane = t & 63;
  const u16* asrc[2];
  const u16* bsrc[3];
#pragma unroll
  for (int i = 0; i < 2; ++i) {
    const int slot = (wv * 2 + i) * 64 + lane;
    const int row = slot >> 3, chs = slot & 7;
    asrc[i] = w2 + (size_t)row * L_ + s0 + 8 * (chs ^ swzB(row));
  }
  const int nb = (wv < 2) ? 3 : 2;
  const int bpiece0 = (wv < 2) ? wv * 3 : 6 + (wv - 2) * 2;
#pragma unroll
  for (int i = 0; i < 3; ++i) {
    const int slot = (bpiece0 + i) * 64 + lane;
    const int row = (slot >> 3) % 80, chs = slot & 7;
    bsrc[i] = G + (size_t)(d0 + row) * F_ + 8 * (chs ^ swzA(row));
  }
  auto issue = [&](int kt) {
    char* st = sm + (kt % E_NST) * E_STAGE;
#pragma unroll
    for (int i = 0; i < 2; ++i) glds16(asrc[i] + (size_t)kt * 64 * L_, st + (wv * 2 + i) * 1024);
#pragma unroll
    for (int i = 0; i < 3; ++i)
      if (i < nb) glds16(bsrc[i] + kt * 64, st + 8192 + (bpiece0 + i) * 1024);
  };
  constexpr int NK = F_ / 64;
#pragma unroll
  for (int s = 0; s < E_NST - 1; ++s) issue(s);
  f32x4 acc[5];
#pragma unroll
  for (int i = 0; i < 5; ++i) acc[i] = (f32x4){0.f, 0.f, 0.f, 0.f};
  const int g = lane >> 4, aq = (lane & 15) >> 2, ap_ = lane & 3;
#pragma unroll 1
  for (int kt = 0; kt < NK; ++kt) {
    if (kt + E_NST - 1 < NK) {
      if (wv < 2) asm volatile("s_waitcnt vmcnt(%0)" ::"n"(5 * (E_NST - 2)) : "memory");
      else asm volatile("s_waitcnt vmcnt(%0)" ::"n"(4 * (E_NST - 2)) : "memory");
    } else {
      asm volatile("s_waitcnt vmcnt(0)" ::: "memory");
    }
    __builtin_amdgcn_s_barrier();
    if (kt + E_NST - 1 < NK) issue(kt + E_NST - 1);
    const char* sa = sm + (kt % E_NST) * E_STAGE;
    const char* sb = sa + 8192;
    bf16x8 af[2], bfg[2][5];
#pragma unroll
    for (int ks = 0; ks < 2; ++ks) {
#pragma unroll
      for (int tt = 0; tt < 2; ++tt) {
        const int krow = 32 * ks + 8 * g + 4 * tt + aq;
        const int chunk = 2 * wv + (ap_ >> 1);
        const bf16x4 x4 = tr_read(sa + krow * 128 + 16 * (chunk ^ swzB(krow)) + 8 * (ap_ & 1));
        af[ks][4 * tt + 0] = x4[0]; af[ks][4 * tt + 1] = x4[1]; af[ks][4 * tt + 2] = x4[2]; af[ks][4 * tt + 3] = x4[3];
      }
#pragma unroll
      for (int n = 0; n < 5; ++n) {
        const int brow = n * 16 + (lane & 15);
        bfg[ks][n] = *reinterpret_cast<const bf16x8*>(sb + brow * 128 + 16 * ((4 * ks + g) ^ swzA(brow)));
      }
    }
    WAIT_LGKM0();
#pragma unroll
    for (int ks = 0; ks < 2; ++ks)
#pragma unroll
      for (int n = 0; n < 5; ++n) acc[n] = __builtin_amdgcn_mfma_f32_16x16x32_bf16(af[ks], bfg[ks][n], acc[n], 0, 0, 0);
  }
  const int sr = s0 + wv * 16 + g * 4;
  float bias[4];
#pragma unroll
  for (int rg = 0; rg < 4; ++rg) bias[rg] = b2f(b2[sr + rg]);
#pragma unroll
  for (int n = 0; n < 5; ++n) {
    const int dd = d0 + n * 16 + (lane & 15);
    const uint2 hv = *reinterpret_cast<const uint2*>(hT + (size_t)dd * L_ + sr);
    const float hh[4] = {lo_f(hv.x), hi_f(hv.x), lo_f(hv.y), hi_f(hv.y)};
#pragma unroll
    for (int rg = 0; rg < 4; ++rg) {
      const float v = bfr(bfr(acc[n][rg]) + bias[rg]);
      out[(size_t)(sr + rg) * D_ + dd] = f2b(hh[rg] + v);
    }
  }
}

extern "C" void kernel_launch(void* const* d_in, const int* in_sizes, int n_in, void* d_out, int out_size, void* d_ws,
                              size_t ws_size, hipStream_t stream) {
  const u16* x = (const u16*)d_in[0];
  const u16* lar = (const u16*)d_in[1];
  const u16* aim = (const u16*)d_in[2];
  const u16* cr = (const u16*)d_in[3];
  const u16* ci = (const u16*)d_in[4];
  const u16* ldt = (const u16*)d_in[5];
  const u16* dsk = (const u16*)d_in[6];
  const u16* w1 = (const u16*)d_in[8];
  const u16* b1 = (const u16*)d_in[9];
  const u16* w2 = (const u16*)d_in[10];
  const u16* b2 = (const u16*)d_in[11];
  char* ws = (char*)d_ws;
  const size_t TS = (size_t)L_ * D_ * 2;
  u16* uT = (u16*)(ws + 0 * TS);
  u16* xT = (u16*)(ws + 1 * TS);
  u16* Kb = (u16*)(ws + 2 * TS);
  u16* hT = (u16*)(ws + 3 * TS);
  u16* hnT = (u16*)(ws + 4 * TS);
  u16* G = (u16*)(ws + 5 * TS);
  kA<<<KA_LN_BLOCKS + D_, 256, 0, stream>>>(x, lar, aim, cr, ci, ldt, uT, xT, Kb);
  kB<<<D_, 256, 0, stream>>>(uT, xT, Kb, dsk, hT);
  kC<<<L_ / 16, 256, 0, stream>>>(hT, hnT);
  kD<<<240, 256, 0, stream>>>(hnT, w1, b1, G);
  kE<<<256, 256, 0, stream>>>(G, w2, b2, hT, (u16*)d_out);
}
```

```cpp
#include <hip/hip_runtime.h>
#include <hip/hip_bf16.h>
#include <stdint.h>

typedef unsigned short u16;
typedef unsigned int u32;
typedef __attribute__((ext_vector_type(8))) short bf16x8;
typedef __attribute__((ext_vector_type(4))) short bf16x4;
typedef __attribute__((ext_vector_type(16))) float f32x16;
typedef __attribute__((ext_vector_type(4))) float f32x4;
typedef __attribute__((ext_vector_type(4))) unsigned int u32x4;

static constexpr int L_ = 2048, D_ = 640, N_ = 192, F_ = 1536;

__device__ __forceinline__ u16 f2b(float f) {
  __hip_bfloat16 b = __float2bfloat16(f);
  return *reinterpret_cast<u16*>(&b);
}
__device__ __forceinline__ float b2f(u16 u) { return __uint_as_float(((u32)u) << 16); }
__device__ __forceinline__ float bfr(float f) { return b2f(f2b(f)); }
__device__ __forceinline__ u32 pack2(float lo, float hi) { return (u32)f2b(lo) | ((u32)f2b(hi) << 16); }
__device__ __forceinline__ float lo_f(u32 w) { return __uint_as_float(w << 16); }
__device__ __forceinline__ float hi_f(u32 w) { return __uint_as_float(w & 0xffff0000u); }

#define STAMP(kid,i) do{}while(0)
struct c32 { float x, y; };
__device__ __forceinline__ c32 cmul(c32 a, c32 b) { return {a.x * b.x - a.y * b.y, a.x * b.y + a.y * b.x}; }
__device__ __forceinline__ c32 csq(c32 a) { return {a.x * a.x - a.y * a.y, 2.f * a.x * a.y}; }

static constexpr int KA_LN_BLOCKS = 128;
static constexpr int KG_STRIDE = 196;
static constexpr int KG_AROWS = 50, KG_BROWS = 64;

__global__ __launch_bounds__(256) void kA(const u16* __restrict__ x, const u16* __restrict__ lar,
                                          const u16* __restrict__ aim, const u16* __restrict__ cr,
                                          const u16* __restrict__ ci, const u16* __restrict__ ldt,
                                          u16* __restrict__ uT, u16* __restrict__ xT, u16* __restrict__ Kb) {
  __shared__ __attribute__((aligned(16))) u32 sm[(KG_AROWS + KG_BROWS) * (KG_STRIDE / 2 + 2) + 704];
  const int t = threadIdx.x;
  if (blockIdx.x < KA_LN_BLOCKS) {
    const int l0 = blockIdx.x * 16;
    const int r = t >> 4, seg = t & 15;
    u32x4 v[5];
#pragma unroll
    for (int i = 0; i < 5; ++i)
      v[i] = *reinterpret_cast<const u32x4*>(x + (size_t)(l0 + r) * D_ + seg * 8 + 128 * i);
    float s = 0.f;
#pragma unroll
    for (int i = 0; i < 5; ++i)
#pragma unroll
      for (int j = 0; j < 4; ++j) s += lo_f(v[i][j]) + hi_f(v[i][j]);
#pragma unroll
    for (int o = 1; o < 16; o <<= 1) s += __shfl_xor(s, o, 64);
    const float mean = s * (1.f / D_);
    float q = 0.f;
#pragma unroll
    for (int i = 0; i < 5; ++i)
#pragma unroll
      for (int j = 0; j < 4; ++j) {
        float a = lo_f(v[i][j]) - mean, b = hi_f(v[i][j]) - mean;
        q += a * a + b * b;
      }
#pragma unroll
    for (int o = 1; o < 16; o <<= 1) q += __shfl_xor(q, o, 64);
    const float mb = bfr(mean);
    const float rden = 1.f / bfr(sqrtf(bfr(bfr(q * (1.f / D_)) + 1e-5f)));
    u16* T = reinterpret_cast<u16*>(sm);
    for (int pass = 0; pass < 2; ++pass) {
#pragma unroll
      for (int i = 0; i < 5; ++i)
#pragma unroll
        for (int j = 0; j < 4; ++j) {
          const int c = seg * 8 + 128 * i + 2 * j;
          u16 e0, e1;
          if (pass == 0) {
            e0 = f2b(bfr(lo_f(v[i][j]) - mb) * rden);
            e1 = f2b(bfr(hi_f(v[i][j]) - mb) * rden);
          } else {
            e0 = (u16)(v[i][j] & 0xffffu);
            e1 = (u16)(v[i][j] >> 16);
          }
          T[c * 24 + r] = e0;
          T[(c + 1) * 24 + r] = e1;
        }
      __syncthreads();
      u16* dst = pass == 0 ? uT : xT;
      for (int idx = t; idx < D_ * 2; idx += 256) {
        const int c = idx >> 1, hh = idx & 1;
        const u32x4 w = *reinterpret_cast<const u32x4*>(T + c * 24 + hh * 8);
        *reinterpret_cast<u32x4*>(dst + (size_t)c * L_ + l0 + hh * 8) = w;
      }
      __syncthreads();
    }
    return;
  }
  const int d = blockIdx.x - KA_LN_BLOCKS;
  u32* Al = sm;
  u32* Bl = sm + KG_AROWS * (KG_STRIDE / 2 + 2);
  float* Kp = reinterpret_cast<float*>(sm + (KG_AROWS + KG_BROWS) * (KG_STRIDE / 2 + 2));
  constexpr int RS = KG_STRIDE / 2 + 2;
  STAMP(0,0);
  u32 ea[41], eb[64];
  if (t < N_) {
    const int gi = d * N_ + t;
    const float dt = bfr(expf(b2f(ldt[d])));
    const float are = -bfr(expf(b2f(lar[gi])));
    const float dre = are * dt, dim = b2f(aim[gi]) * dt;
    float sn, cs;
    __sincosf(dim, &sn, &cs);
    const float er = __expf(dre);
    c32 z1 = {er * cs, er * sn};
    c32 C = {2.f * b2f(cr[gi]) * dt, 2.f * b2f(ci[gi]) * dt};
    c32 z2 = csq(z1), z4 = csq(z2), z8 = csq(z4), z16 = csq(z8), z32 = csq(z16), z64 = csq(z32), z128 = csq(z64),
        z256 = csq(z128), z512 = csq(z256), z1024 = csq(z512);
    {
      c32 base[4] = {z1, z2, z4, z8};
#pragma unroll
      for (int sg = 0; sg < 4; ++sg) {
        c32 w = {1.f, 0.f};
#pragma unroll
        for (int b = 0; b < 16; ++b) {
          eb[sg * 16 + b] = pack2(w.x, w.y);
          if (b < 15) w = cmul(w, base[sg]);
        }
      }
    }
    {
      c32 w = C;
#pragma unroll
      for (int a = 0; a < 16; ++a) { ea[a] = pack2(w.x, -w.y); w = cmul(w, z16); }
      w = cmul(C, z256);
#pragma unroll
      for (int a = 0; a < 8; ++a) { ea[16 + a] = pack2(w.x, -w.y); w = cmul(w, z32); }
      w = cmul(C, z512);
#pragma unroll
      for (int a = 0; a < 8; ++a) { ea[24 + a] = pack2(w.x, -w.y); w = cmul(w, z64); }
      w = cmul(C, z1024);
#pragma unroll
      for (int a = 0; a < 9; ++a) { ea[32 + a] = pack2(w.x, -w.y); w = cmul(w, z128); }
    }
  }
  STAMP(0,1);
  const int wv = t >> 6, lane = t & 63;
  f32x4 acc = {0.f, 0.f, 0.f, 0.f};
  const int arow0 = (wv == 0) ? 0 : (16 + (wv - 1) * 9);
  for (int ph = 0; ph < 2; ++ph) {
    if (t < N_ && (t / 96) == ph) {
      const int nn = t % 96;
#pragma unroll
      for (int a = 0; a < 16; ++a) Al[a * RS + nn] = ea[a];
#pragma unroll
      for (int a = 0; a < 8; ++a) Al[(16 + a) * RS + nn] = ea[16 + a];
#pragma unroll
      for (int a = 0; a < 8; ++a) Al[(25 + a) * RS + nn] = ea[24 + a];
#pragma unroll
      for (int a = 0; a < 9; ++a) Al[(34 + a) * RS + nn] = ea[32 + a];
#pragma unroll
      for (int b = 0; b < 64; ++b) Bl[b * RS + nn] = eb[b];
    }
    __syncthreads();
#pragma unroll
    for (int ks = 0; ks < 6; ++ks) {
      const bf16x8 af = *reinterpret_cast<const bf16x8*>(Al + (arow0 + (lane & 15)) * RS + ks * 16 + (lane >> 4) * 4);
      const bf16x8 bfg = *reinterpret_cast<const bf16x8*>(Bl + (wv * 16 + (lane & 15)) * RS + ks * 16 + (lane >> 4) * 4);
      acc = __builtin_amdgcn_mfma_f32_16x16x32_bf16(af, bfg, acc, 0, 0, 0);
    }
    __syncthreads();
  }
  STAMP(0,2);
  {
    const int b = lane & 15;
#pragma unroll
    for (int rg = 0; rg < 4; ++rg) {
      const int a = (lane >> 4) * 4 + rg;
      if (wv == 0) Kp[16 * a + b] = acc[rg];
      else if (a < 8 || (wv == 3 && a == 8 && b == 0)) Kp[256 + (wv - 1) * 128 + 16 * a + b] = acc[rg];
    }
  }
  __syncthreads();
  {
    u32 o[4];
#pragma unroll
    for (int j = 0; j < 4; ++j) {
      float kv[2];
#pragma unroll
      for (int e = 0; e < 2; ++e) {
        const int l = 8 * t + 2 * j + e;
        const int p = (int)bfr((float)l);
        int idx;
        if (p < 256) idx = p;
        else if (p < 512) idx = 256 + ((p - 256) >> 1);
        else if (p < 1024) idx = 384 + ((p - 512) >> 2);
        else idx = 512 + ((p - 1024) >> 3);
        kv[e] = Kp[idx];
      }
      o[j] = pack2(kv[0], kv[1]);
    }
    u32x4 w = {o[0], o[1], o[2], o[3]};
    *reinterpret_cast<u32x4*>(Kb + (size_t)d * L_ + 8 * t) = w;
  }
  STAMP(0,3);
}

static constexpr int KB_CS = 4608;
static constexpr int KB_UOFF = 8 * KB_CS;
static constexpr int KB_USTR = 144;
static constexpr int KB_LDS = KB_UOFF + 33 * KB_USTR;

__global__ __launch_bounds__(256) void kB(const u16* __restrict__ uT, const u16* __restrict__ xT,
                                          const u16* __restrict__ Kb, const u16* __restrict__ dsk,
                                          u16* __restrict__ hT) {
  __shared__ __attribute__((aligned(16))) char sm[KB_LDS];
  const int d = blockIdx.x, t = threadIdx.x, wv = t >> 6, lane = t & 63;
  auto cbase = [](int c) -> int {
    const int o = (c == 0) ? 0 : (c == 1) ? 1 : (c == 2 || c == 5) ? 5 : (c == 3 || c == 6) ? 9 : 13;
    return c * KB_CS + 16 * o;
  };
  STAMP(1,0);
  const u32x4 uv = *reinterpret_cast<const u32x4*>(uT + (size_t)d * L_ + 8 * t);
  *reinterpret_cast<u32x4*>(sm + KB_UOFF + KB_USTR * (1 + (t >> 3)) + (t & 7) * 16) = uv;
  if (t < 9) {
    u32x4 zz = {0u, 0u, 0u, 0u};
    *reinterpret_cast<u32x4*>(sm + KB_UOFF + t * 16) = zz;
  }
  for (int blk = t; blk < 264; blk += 256) {
    u32x4 c1 = {0u, 0u, 0u, 0u}, c2 = {0u, 0u, 0u, 0u};
    u32 e0 = 0u;
    const int i1 = 2040 - 8 * blk, i2 = 2032 - 8 * blk;
    if (i1 >= 0) c1 = *reinterpret_cast<const u32x4*>(Kb + (size_t)d * L_ + i1);
    if (i2 >= 0) c2 = *reinterpret_cast<const u32x4*>(Kb + (size_t)d * L_ + i2);
    if (blk > 0 && blk <= 256) e0 = (u32)Kb[(size_t)d * L_ + 2048 - 8 * blk];
    u32 w[8];
    w[0] = (e0 & 0xffffu) | (c1[3] & 0xffff0000u);
    w[1] = (c1[3] & 0xffffu) | (c1[2] & 0xffff0000u);
    w[2] = (c1[2] & 0xffffu) | (c1[1] & 0xffff0000u);
    w[3] = (c1[1] & 0xffffu) | (c1[0] & 0xffff0000u);
    w[4] = (c1[0] & 0xffffu) | (c2[3] & 0xffff0000u);
    w[5] = (c2[3] & 0xffffu) | (c2[2] & 0xffff0000u);
    w[6] = (c2[2] & 0xffffu) | (c2[1] & 0xffff0000u);
    w[7] = (c2[1] & 0xffffu) | (c2[0] & 0xffff0000u);
#pragma unroll
    for (int c = 0; c < 8; ++c) {
      u32x4 o;
      const int e = c >> 1;
      if ((c & 1) == 0) {
        o[0] = w[e]; o[1] = w[e + 1]; o[2] = w[e + 2]; o[3] = w[e + 3];
      } else {
#pragma unroll
        for (int i = 0; i < 4; ++i) o[i] = __builtin_amdgcn_alignbit(w[e + i + 1], w[e + i], 16);
      }
      *reinterpret_cast<u32x4*>(sm + cbase(c) + blk * 16) = o;
    }
  }
  STAMP(1,1);
  __syncthreads();
  STAMP(1,2);
  const int r = lane & 31, h = lane >> 5;
  const int cc = (8 - (r & 7)) & 7, g8 = (r + 7) & ~7;
  const int abase = cbase(cc) + 2 * (2048 + 8 * h - g8) - 64;
  f32x16 acc0 = {}, acc1 = {};
#pragma unroll 1
  for (int dl = wv; dl < 32; dl += 4) {
    const char* ap = sm + abase - 128 * dl;
    int ch = r - dl;
    ch = ch < -1 ? -1 : ch;
    const char* bp = sm + KB_UOFF + KB_USTR * (ch + 1) + 16 * h;
#pragma unroll
    for (int kk = 0; kk < 4; ++kk) {
      const bf16x8 bfr_ = *reinterpret_cast<const bf16x8*>(bp + 32 * kk);
      const bf16x8 a1 = *reinterpret_cast<const bf16x8*>(ap + 32 * kk);
      const bf16x8 a0 = *reinterpret_cast<const bf16x8*>(ap + 64 + 32 * kk);
      acc0 = __builtin_amdgcn_mfma_f32_32x32x16_bf16(a0, bfr_, acc0, 0, 0, 0);
      acc1 = __builtin_amdgcn_mfma_f32_32x32x16_bf16(a1, bfr_, acc1, 0, 0, 0);
    }
  }
  STAMP(1,3);
  __syncthreads();
  float* red = reinterpret_cast<float*>(sm);
  constexpr int RST = 33;
#pragma unroll
  for (int rg = 0; rg < 16; ++rg) {
    const int row = (rg & 3) + 8 * (rg >> 2) + 4 * h;
    red[(wv * 64 + row) * RST + r] = acc0[rg];
    red[(wv * 64 + 32 + row) * RST + r] = acc1[rg];
  }
  __syncthreads();
  {
    const int a = t >> 3, b0 = (t & 7) * 8;
    const u32x4 xv = *reinterpret_cast<const u32x4*>(xT + (size_t)d * L_ + 8 * t);
    const float ds = b2f(dsk[d]);
    u32 o[4];
#pragma unroll
    for (int j = 0; j < 4; ++j) {
      float hv[2];
#pragma unroll
      for (int e = 0; e < 2; ++e) {
        const int b = b0 + 2 * j + e;
        float y = red[(0 * 64 + b) * RST + a] + red[(1 * 64 + b) * RST + a] + red[(2 * 64 + b) * RST + a] +
                  red[(3 * 64 + b) * RST + a];
        const float uu = e ? hi_f(uv[j]) : lo_f(uv[j]);
        const float xx = e ? hi_f(xv[j]) : lo_f(xv[j]);
        const float r2 = bfr(bfr(y) + bfr(uu * ds));
        hv[e] = xx + r2;
      }
      o[j] = pack2(hv[0], hv[1]);
    }
    u32x4 w = {o[0], o[1], o[2], o[3]};
    *reinterpret_cast<u32x4*>(hT + (size_t)d * L_ + 8 * t) = w;
  }
  STAMP(1,4);
}

__global__ __launch_bounds__(256) void kC(const u16* __restrict__ hT, u16* __restrict__ hnT) {
  __shared__ float red[4][16];
  const int l0 = blockIdx.x * 16, t = threadIdx.x, hf = t & 1, dr = t >> 1, wv = t >> 6;
  float v[5][8];
#pragma unroll
  for (int i = 0; i < 5; ++i) {
    const u32x4 w = *reinterpret_cast<const u32x4*>(hT + (size_t)(dr + 128 * i) * L_ + l0 + hf * 8);
#pragma unroll
    for (int j = 0; j < 4; ++j) { v[i][2 * j] = lo_f(w[j]); v[i][2 * j + 1] = hi_f(w[j]); }
  }
#pragma unroll
  for (int pass = 0; pass < 2; ++pass) {
    float s[8];
#pragma unroll
    for (int j = 0; j < 8; ++j) {
      s[j] = v[0][j] + v[1][j] + v[2][j] + v[3][j] + v[4][j];
#pragma unroll
      for (int o = 2; o < 64; o <<= 1) s[j] += __shfl_xor(s[j], o, 64);
    }
    if ((t & 63) < 2)
#pragma unroll
      for (int j = 0; j < 8; ++j) red[wv][hf * 8 + j] = s[j];
    __syncthreads();
    float mean[8];
#pragma unroll
    for (int j = 0; j < 8; ++j)
      mean[j] = (red[0][hf * 8 + j] + red[1][hf * 8 + j] + red[2][hf * 8 + j] + red[3][hf * 8 + j]) * (1.f / D_);
    __syncthreads();
#pragma unroll
    for (int j = 0; j < 8; ++j) {
      float q = 0.f;
#pragma unroll
      for (int i = 0; i < 5; ++i) { const float dd = v[i][j] - mean[j]; q += dd * dd; }
#pragma unroll
      for (int o = 2; o < 64; o <<= 1) q += __shfl_xor(q, o, 64);
      s[j] = q;
    }
    if ((t & 63) < 2)
#pragma unroll
      for (int j = 0; j < 8; ++j) red[wv][hf * 8 + j] = s[j];
    __syncthreads();
#pragma unroll
    for (int j = 0; j < 8; ++j) {
      const float var = (red[0][hf * 8 + j] + red[1][hf * 8 + j] + red[2][hf * 8 + j] + red[3][hf * 8 + j]) * (1.f / D_);
      const float mb = bfr(mean[j]);
      const float rden = 1.f / bfr(sqrtf(bfr(bfr(var) + 1e-5f)));
#pragma unroll
      for (int i = 0; i < 5; ++i) v[i][j] = bfr(bfr(v[i][j] - mb) * rden);
    }
    __syncthreads();
  }
#pragma unroll
  for (int i = 0; i < 5; ++i) {
    u32x4 w;
#pragma unroll
    for (int j = 0; j < 4; ++j) w[j] = pack2(v[i][2 * j], v[i][2 * j + 1]);
    *reinterpret_cast<u32x4*>(hnT + (size_t)(dr + 128 * i) * L_ + l0 + hf * 8) = w;
  }
}

__device__ __forceinline__ void glds16(const void* g, void* l) {
  __builtin_amdgcn_global_load_lds(reinterpret_cast<const u32*>(g), reinterpret_cast<u32*>(l), 16, 0, 0);
}
__device__ __forceinline__ bf16x4 tr_read(const char* p) {
  bf16x4 r;
  const u32 a = (u32)(size_t)p;
  asm volatile("ds_read_b64_tr_b16 %0, %1" : "=v"(r) : "v"(a) : "memory");
  return r;
}
#define WAIT_LGKM0() asm volatile("s_waitcnt lgkmcnt(0)" ::: "memory")
__device__ __forceinline__ int swzA(int row) { return (row >> 1) & 7; }
__device__ __forceinline__ int swzB(int row) { return (((row >> 1) & 1) << 2) | (((row >> 3) & 1) << 1); }

__device__ __forceinline__ float gelu_tanh(float x) {
  const float y = 0.7978845608028654f * (x + 0.044715f * x * x * x);
  const float e = __expf(2.f * y);
  const float th = 1.f - 2.f / (e + 1.f);
  return 0.5f * x * (1.f + th);
}

static constexpr int D_NST = 6;
__global__ __launch_bounds__(256) void kD(const u16* __restrict__ hnT, const u16* __restrict__ w1,
                                          const u16* __restrict__ b1, u16* __restrict__ G) {
  __shared__ __attribute__((aligned(16))) char sm[D_NST * 16384];
  const int bid = blockIdx.x;
  const int xcd = bid & 7, q = bid >> 3;
  const int ft = xcd * 3 + q / 10, dtile = q % 10;
  const int d0 = dtile * 64, f0 = ft * 64;
  const int t = threadIdx.x, wv = t >> 6, lane = t & 63;
  const int wr = wv >> 1, wc = wv & 1;
  const u16* asrc[2];
  const u16* bsrc[2];
#pragma unroll
  for (int i = 0; i < 2; ++i) {
    const int slot = (wv * 2 + i) * 64 + lane;
    const int row = slot >> 3, chs = slot & 7;
    asrc[i] = hnT + (size_t)(d0 + row) * L_ + 8 * (chs ^ swzA(row));
    bsrc[i] = w1 + (size_t)row * F_ + f0 + 8 * (chs ^ swzB(row));
  }
  auto issue = [&](int kt) {
    char* st = sm + (kt % D_NST) * 16384;
#pragma unroll
    for (int i = 0; i < 2; ++i) {
      glds16(asrc[i] + kt * 64, st + (wv * 2 + i) * 1024);
      glds16(bsrc[i] + (size_t)kt * 64 * F_, st + 8192 + (wv * 2 + i) * 1024);
    }
  };
  constexpr int NK = L_ / 64;
#pragma unroll
  for (int s = 0; s < D_NST - 1; ++s) issue(s);
  f32x16 acc = {};
  const int r = lane & 31, h = lane >> 5;
  const int arow = wr * 32 + r;
  const int bq = (lane & 15) >> 2, bp_ = lane & 3, bblk = (lane >> 4) & 1;
#pragma unroll 1
  for (int kt = 0; kt < NK; ++kt) {
    if (kt + D_NST - 1 < NK) {
      asm volatile("s_waitcnt vmcnt(%0)" ::"n"(4 * (D_NST - 2)) : "memory");
    } else {
      asm volatile("s_waitcnt vmcnt(0)" ::: "memory");
    }
    __builtin_amdgcn_s_barrier();
    if (kt + D_NST - 1 < NK) issue(kt + D_NST - 1);
    const char* sa = sm + (kt % D_NST) * 16384;
    const char* sb = sa + 8192;
    bf16x8 af[4], bfg[4];
    bf16x4 tq[4][2];
#pragma unroll
    for (int ks = 0; ks < 4; ++ks) {
      af[ks] = *reinterpret_cast<const bf16x8*>(sa + arow * 128 + 16 * ((2 * ks + h) ^ swzA(arow)));
#pragma unroll
      for (int tt = 0; tt < 2; ++tt) {
        const int krow = 16 * ks + 8 * h + 4 * tt + bq;
        const int chunk = 4 * wc + 2 * bblk + (bp_ >> 1);
        tq[ks][tt] = tr_read(sb + krow * 128 + 16 * (chunk ^ swzB(krow)) + 8 * (bp_ & 1));
      }
    }
    asm volatile("s_waitcnt lgkmcnt(0)" : "+v"(tq[0][0]), "+v"(tq[0][1]), "+v"(tq[1][0]), "+v"(tq[1][1]), "+v"(tq[2][0]),
                 "+v"(tq[2][1]), "+v"(tq[3][0]), "+v"(tq[3][1])::"memory");
#pragma unroll
    for (int ks = 0; ks < 4; ++ks)
#pragma unroll
      for (int e = 0; e < 4; ++e) { bfg[ks][e] = tq[ks][0][e]; bfg[ks][4 + e] = tq[ks][1][e]; }
#pragma unroll
    for (int ks = 0; ks < 4; ++ks) acc = __builtin_amdgcn_mfma_f32_32x32x16_bf16(af[ks], bfg[ks], acc, 0, 0, 0);
  }
  const int f = f0 + wc * 32 + r;
  const float bias = b2f(b1[f]);
#pragma unroll
  for (int rg = 0; rg < 16; ++rg) {
    const int dd = d0 + wr * 32 + (rg & 3) + 8 * (rg >> 2) + 4 * h;
    const float v = bfr(bfr(acc[rg]) + bias);
    G[(size_t)dd * F_ + f] = f2b(gelu_tanh(v));
  }
}

static constexpr int E_NST = 6;
static constexpr int E_STAGE = 8192 + 10240;
__global__ __launch_bounds__(256) void kE(const u16* __restrict__ G, const u16* __restrict__ w2,
                                          const u16* __restrict__ b2, const u16* __restrict__ hT,
                                          u16* __restrict__ out) {
  __shared__ __attribute__((aligned(16))) char sm[E_NST * E_STAGE];
  const int bid = blockIdx.x;
  const int xcd = bid & 7, q = bid >> 3;
  const int stile = xcd * 4 + (q >> 3), dtile = q & 7;
  const int s0 = stile * 64, d0 = dtile * 80;
  const int t = threadIdx.x, wv = t >> 6, lane = t & 63;
  const u16* asrc[2];
  const u16* bsrc[3];
#pragma unroll
  for (int i = 0; i < 2; ++i) {
    const int slot = (wv * 2 + i) * 64 + lane;
    const int row = slot >> 3, chs = slot & 7;
    asrc[i] = w2 + (size_t)row * L_ + s0 + 8 * (chs ^ swzB(row));
  }
  const int nb = (wv < 2) ? 3 : 2;
  const int bpiece0 = (wv < 2) ? wv * 3 : 6 + (wv - 2) * 2;
#pragma unroll
  for (int i = 0; i < 3; ++i) {
    const int slot = (bpiece0 + i) * 64 + lane;
    const int row = (slot >> 3) % 80, chs = slot & 7;
    bsrc[i] = G + (size_t)(d0 + row) * F_ + 8 * (chs ^ swzA(row));
  }
  auto issue = [&](int kt) {
    char* st = sm + (kt % E_NST) * E_STAGE;
#pragma unroll
    for (int i = 0; i < 2; ++i) glds16(asrc[i] + (size_t)kt * 64 * L_, st + (wv * 2 + i) * 1024);
#pragma unroll
    for (int i = 0; i < 3; ++i)
      if (i < nb) glds16(bsrc[i] + kt * 64, st + 8192 + (bpiece0 + i) * 1024);
  };
  constexpr int NK = F_ / 64;
#pragma unroll
  for (int s = 0; s < E_NST - 1; ++s) issue(s);
  f32x4 acc[5];
#pragma unroll
  for (int i = 0; i < 5; ++i) acc[i] = (f32x4){0.f, 0.f, 0.f, 0.f};
  const int g = lane >> 4, aq = (lane & 15) >> 2, ap_ = lane & 3;
#pragma unroll 1
  for (int kt = 0; kt < NK; ++kt) {
    if (kt + E_NST - 1 < NK) {
      if (wv < 2) asm volatile("s_waitcnt vmcnt(%0)" ::"n"(5 * (E_NST - 2)) : "memory");
      else asm volatile("s_waitcnt vmcnt(%0)" ::"n"(4 * (E_NST - 2)) : "memory");
    } else {
      asm volatile("s_waitcnt vmcnt(0)" ::: "memory");
    }
    __builtin_amdgcn_s_barrier();
    if (kt + E_NST - 1 < NK) issue(kt + E_NST - 1);
    const char* sa = sm + (kt % E_NST) * E_STAGE;
    const char* sb = sa + 8192;
    bf16x8 af[2], bfg[2][5];
    bf16x4 tq[2][2];
#pragma unroll
    for (int ks = 0; ks < 2; ++ks) {
#pragma unroll
      for (int tt = 0; tt < 2; ++tt) {
        const int krow = 32 * ks + 8 * g + 4 * tt + aq;
        const int chunk = 2 * wv + (ap_ >> 1);
        tq[ks][tt] = tr_read(sa + krow * 128 + 16 * (chunk ^ swzB(krow)) + 8 * (ap_ & 1));
      }
#pragma unroll
      for (int n = 0; n < 5; ++n) {
        const int brow = n * 16 + (lane & 15);
        bfg[ks][n] = *reinterpret_cast<const bf16x8*>(sb + brow * 128 + 16 * ((4 * ks + g) ^ swzA(brow)));
      }
    }
    asm volatile("s_waitcnt lgkmcnt(0)" : "+v"(tq[0][0]), "+v"(tq[0][1]), "+v"(tq[1][0]), "+v"(tq[1][1])::"memory");
#pragma unroll
    for (int ks = 0; ks < 2; ++ks)
#pragma unroll
      for (int e = 0; e < 4; ++e) { af[ks][e] = tq[ks][0][e]; af[ks][4 + e] = tq[ks][1][e]; }
#pragma unroll
    for (int ks = 0; ks < 2; ++ks)
#pragma unroll
      for (int n = 0; n < 5; ++n) acc[n] = __builtin_amdgcn_mfma_f32_16x16x32_bf16(af[ks], bfg[ks][n], acc[n], 0, 0, 0);
  }
  const int sr = s0 + wv * 16 + g * 4;
  float bias[4];
#pragma unroll
  for (int rg = 0; rg < 4; ++rg) bias[rg] = b2f(b2[sr + rg]);
#pragma unroll
  for (int n = 0; n < 5; ++n) {
    const int dd = d0 + n * 16 + (lane & 15);
    const uint2 hv = *reinterpret_cast<const uint2*>(hT + (size_t)dd * L_ + sr);
    const float hh[4] = {lo_f(hv.x), hi_f(hv.x), lo_f(hv.y), hi_f(hv.y)};
#pragma unroll
    for (int rg = 0; rg < 4; ++rg) {
      const float v = bfr(bfr(acc[n][rg]) + bias[rg]);
      out[(size_t)(sr + rg) * D_ + dd] = f2b(hh[rg] + v);
    }
  }
}

extern "C" void kernel_launch(void* const* d_in, const int* in_sizes, int n_in, void* d_out, int out_size, void* d_ws,
                              size_t ws_size, hipStream_t stream) {
  const u16* x = (const u16*)d_in[0];
  const u16* lar = (const u16*)d_in[1];
  const u16* aim = (const u16*)d_in[2];
  const u16* cr = (const u16*)d_in[3];
  const u16* ci = (const u16*)d_in[4];
  const u16* ldt = (const u16*)d_in[5];
  const u16* dsk = (const u16*)d_in[6];
  const u16* w1 = (const u16*)d_in[8];
  const u16* b1 = (const u16*)d_in[9];
  const u16* w2 = (const u16*)d_in[10];
  const u16* b2 = (const u16*)d_in[11];
  char* ws = (char*)d_ws;
  const size_t TS = (size_t)L_ * D_ * 2;
  u16* uT = (u16*)(ws + 0 * TS);
  u16* xT = (u16*)(ws + 1 * TS);
  u16* Kb = (u16*)(ws + 2 * TS);
  u16* hT = (u16*)(ws + 3 * TS);
  u16* hnT = (u16*)(ws + 4 * TS);
  u16* G = (u16*)(ws + 5 * TS);
  kA<<<KA_LN_BLOCKS + D_, 256, 0, stream>>>(x, lar, aim, cr, ci, ldt, uT, xT, Kb);
  kB<<<D_, 256, 0, stream>>>(uT, xT, Kb, dsk, hT);
  kC<<<L_ / 16, 256, 0, stream>>>(hT, hnT);
  kD<<<240, 256, 0, stream>>>(hnT, w1, b1, G);
  kE<<<256, 256, 0, stream>>>(G, w2, b2, hT, (u16*)d_out);
}
```
